```python
import torch

def _softmax(x, axis=-1):
    return torch.softmax(x, dim=axis)
def _gelu(x):
    return torch.nn.functional.gelu(x, approximate="tanh")
def _silu(x):
    return torch.nn.functional.silu(x)
def _sigmoid(x):
    return torch.sigmoid(x)
def _layernorm(x, axis=-1, eps=1e-5):
    m = x.mean(dim=axis, keepdim=True)
    v = x.var(dim=axis, keepdim=True, unbiased=False)
    return (x - m) / torch.sqrt(v + eps)
def _scatter_add(tgt, idx, src, axis=0):
    return tgt.clone().index_add_(axis, idx, src)
def _segment_sum(src, seg, n):
    out = torch.zeros((n,) + src.shape[1:], dtype=src.dtype)
    return out.index_add_(0, seg, src)
def _pad(x, pw):
    flat = tuple(v for pair in reversed(pw) for v in pair)
    return torch.nn.functional.pad(x, flat)

kernel_name = 'gated_mix__ssm_conv_h3__softmax_full_mha__prompt'

def setup_inputs():

    _w = (lambda a: (lambda t: t.to(torch.bfloat16) if t.is_floating_point() else t)(torch.as_tensor(a)))
    def _A_setup():
        B, S, D = 1, 2048, 512
        ks = 4
        return {
            "q": _w((lambda *s: torch.randn(*s, dtype=torch.bfloat16) * 0.05)(B, S, D)),
            "k": _w((lambda *s: torch.randn(*s, dtype=torch.bfloat16) * 0.4)(B, S, D)),
            "v": _w((lambda *s: torch.randn(*s, dtype=torch.bfloat16) * 0.4)(B, S, D)),
            "shift_k": _w((lambda *s: torch.randn(*s, dtype=torch.bfloat16) * 0.4)(D, ks) * 0.5),
            "ssm_k": _w((lambda *s: torch.randn(*s, dtype=torch.bfloat16) * 1.6)(D, S) * (1.0 / S ** 0.5)),
            "ssm_D": _w((lambda *s: torch.randn(*s, dtype=torch.bfloat16) * 0.4)(D)),
        }
    _A_in = _A_setup()

    def _B_setup():
        B, S, D = 1, 2048, 512
        return {
            "x": _w((lambda *s: torch.randn(*s, dtype=torch.bfloat16) * 0.05)(B, S, D)),
            "wqkv": _w((lambda *s: torch.randn(*s, dtype=torch.bfloat16) * 0.8)(D, 3 * D)),
            "wo": _w((lambda *s: torch.randn(*s, dtype=torch.bfloat16) * 0.05)(D, D)),
        }
    _B_in = _B_setup()

    return {
        "A_q": _A_in["q"],
        "A_k": _A_in["k"],
        "A_v": _A_in["v"],
        "A_shift_k": _A_in["shift_k"],
        "A_ssm_k": _A_in["ssm_k"],
        "A_ssm_D": _A_in["ssm_D"],
        "B_x": _B_in["x"],
        "B_wqkv": _B_in["wqkv"],
        "B_wo": _B_in["wo"],
        "gate_w": _w((lambda *s: torch.randn(*s, dtype=torch.bfloat16) * 0.05)(512) * 16.0),
    }

def reference(A_q, A_k, A_v, A_shift_k, A_ssm_k, A_ssm_D, B_x, B_wqkv, B_wo, gate_w):
    def _A_ref(q, k, v, shift_k, ssm_k, ssm_D):
        B, S, D = q.shape
        ks = shift_k.shape[-1]
        k_pad = _pad(k, ((0, 0), (ks - 1, 0), (0, 0)))
        k_shift = torch.zeros_like(k)
        for t in range(ks):
            k_shift = k_shift + k_pad[:, t:t + S, :] * shift_k[:, ks - 1 - t][None, None, :]
        kv = k_shift * v
        L = 2 * S
        kv_pad = _pad(kv, ((0, 0), (0, S), (0, 0)))
        kf_pad = _pad(ssm_k, ((0, 0), (0, S)))
        kv_f = (lambda x, axes=(-2,-1): torch.fft.rfft2(x.float(), dim=axes))(kv_pad[:, None, :, :], axes=(1, 2))
        kf_f = (lambda x, axes=(-2,-1): torch.fft.rfft2(x.float(), dim=axes))(kf_pad[:, None, :], axes=(1, 2))
        kf_f = (lambda x, *d: x.permute(*d))(kf_f, 1, 2, 0)
        y_f = kv_f * kf_f[None, :, :, :]
        y = (lambda x, s, axes=(-2,-1): torch.fft.irfft2(x, s=s, dim=axes).to(torch.bfloat16))(y_f, (1, L), axes=(1, 2))[:, 0, :S, :]
        y = y + kv * ssm_D[None, None, :]
        return q * y

    def _B_ref(x, wqkv, wo):
        B, S, D = x.shape
        H = 32; Dh = D // H
        qkv = (lambda x, *d: x.permute(*d))((x @ wqkv).reshape(B, S, 3, H, Dh), 2, 0, 3, 1, 4)
        q, k, v = qkv[0], qkv[1], qkv[2]
        scale = Dh ** -0.5
        scores = torch.einsum("bhqd,bhkd->bhqk", q, k) * scale
        p = _softmax(scores, axis=-1)
        o = torch.einsum("bhqk,bhkd->bhqd", p, v)
        return (lambda x, *d: x.permute(*d))(o, 0, 2, 1, 3).reshape(B, S, D) @ wo

    x = A_q
    g = _sigmoid((x * gate_w).sum(-1, keepdims=True))
    a = _A_ref(x, A_k, A_v, A_shift_k, A_ssm_k, A_ssm_D)
    b = _B_ref(x, B_wqkv, B_wo)
    return g * a + (1 - g) * b
```

```cpp
#include <hip/hip_runtime.h>
#include <hip/hip_bf16.h>
#include <stdint.h>

typedef __attribute__((ext_vector_type(8))) short bf16x8;
typedef __attribute__((ext_vector_type(4))) short s16x4;
typedef __attribute__((ext_vector_type(16))) float f32x16;
typedef __attribute__((ext_vector_type(4))) unsigned u32x4;
typedef unsigned short u16;

#define DI __device__ __forceinline__
#define MFMA32(a, b, c) __builtin_amdgcn_mfma_f32_32x32x16_bf16((a), (b), (c), 0, 0, 0)

constexpr int S = 2048;
constexpr int D = 512;
constexpr int H = 32;
constexpr int DH = 16;

DI u16 f2bf(float x) {
  unsigned u = __float_as_uint(x);
  u += 0x7fffu + ((u >> 16) & 1u);
  return (u16)(u >> 16);
}
DI float bf2f(u16 b) { return __uint_as_float(((unsigned)b) << 16); }
DI float rbf(float x) { return bf2f(f2bf(x)); }

DI unsigned comp4(const uint4& v, int i) { return i == 0 ? v.x : i == 1 ? v.y : i == 2 ? v.z : v.w; }
DI u16 ext8(const uint4& v, int e) { unsigned w = comp4(v, e >> 1); return (u16)((e & 1) ? (w >> 16) : (w & 0xffffu)); }
DI u16 ext4(const uint2& v, int e) { unsigned w = (e >> 1) ? v.y : v.x; return (u16)((e & 1) ? (w >> 16) : (w & 0xffffu)); }
DI unsigned pack2(float lo, float hi) { return (unsigned)f2bf(lo) | ((unsigned)f2bf(hi) << 16); }

typedef __attribute__((address_space(3))) s16x4* lds_s16x4_ptr;
typedef __attribute__((address_space(3))) const bf16x8* lds_bf16x8_ptr;
DI s16x4 tr_read(unsigned lds_addr) {
  return __builtin_amdgcn_ds_read_tr16_b64_v4i16((lds_s16x4_ptr)(size_t)lds_addr);
}

constexpr size_t OFF_Q = 0;
constexpr size_t OFF_K = OFF_Q + (size_t)H * S * DH * 2;
constexpr size_t OFF_V = OFF_K + (size_t)H * S * DH * 2;
constexpr size_t OFF_KVT = OFF_V + (size_t)H * S * DH * 2;
constexpr size_t OFF_ZT = OFF_KVT + (size_t)D * S * 2;
constexpr size_t OFF_O = OFF_ZT + (size_t)D * S * 2;
constexpr size_t OFF_G = OFF_O + (size_t)S * D * 2;

constexpr int K1_GEMM_BLOCKS = 192;
constexpr int K1_PREP_BLOCKS = 64;
constexpr int A_STRIDE = 144;
constexpr int B_STRIDE = 320;

__global__ __launch_bounds__(256) void k1_qkv_prep(const u16* __restrict__ x, const u16* __restrict__ kin,
                                                  const u16* __restrict__ vin, const u16* __restrict__ shift_k,
                                                  const u16* __restrict__ wqkv, const u16* __restrict__ gate_w,
                                                  char* __restrict__ ws) {
  __shared__ __attribute__((aligned(16))) char smem[128 * A_STRIDE + 64 * B_STRIDE];
  const int tid = threadIdx.x;
  const int lane = tid & 63, wid = tid >> 6;
  const int bid = blockIdx.x;
  if (bid < K1_GEMM_BLOCKS) {
    const int bm = bid / 12, bn = bid % 12;
    char* sA = smem;
    char* sB = smem + 128 * A_STRIDE;
    const int wm = wid >> 1, wn = wid & 1;
    const int r32 = lane & 31, hh = lane >> 5;
    f32x16 acc[2][2];
#pragma unroll
    for (int i = 0; i < 2; ++i)
#pragma unroll
      for (int j = 0; j < 2; ++j)
#pragma unroll
        for (int e = 0; e < 16; ++e) acc[i][j][e] = 0.f;
    const bool isV = (bn >= 8);
    for (int k0 = 0; k0 < D; k0 += 64) {
      u32x4 ra[4], rb[4];
#pragma unroll
      for (int i = 0; i < 4; ++i) {
        int c = tid + 256 * i;
        int row = c >> 3, kc = c & 7;
        ra[i] = *(const u32x4*)(x + (size_t)(bm * 128 + row) * D + k0 + kc * 8);
        int krow = c >> 4, nc = c & 15;
        rb[i] = *(const u32x4*)(wqkv + (size_t)(k0 + krow) * (3 * D) + bn * 128 + nc * 8);
      }
      __syncthreads();
#pragma unroll
      for (int i = 0; i < 4; ++i) {
        int c = tid + 256 * i;
        int row = c >> 3, kc = c & 7;
        *(u32x4*)(sA + row * A_STRIDE + kc * 16) = ra[i];
        int krow = c >> 4, nc = c & 15;
        *(u32x4*)(sB + krow * B_STRIDE + nc * 16) = rb[i];
      }
      __syncthreads();
#pragma unroll
      for (int ks = 0; ks < 4; ++ks) {
        bf16x8 af[2], bfr[2];
#pragma unroll
        for (int mi = 0; mi < 2; ++mi) {
          int row = wm * 64 + mi * 32 + r32;
          af[mi] = *(const bf16x8*)(sA + row * A_STRIDE + (ks * 16 + 8 * hh) * 2);
        }
        const int i16 = lane & 15, q4 = i16 >> 2, p4 = i16 & 3, g = lane >> 4;
#pragma unroll
        for (int ni = 0; ni < 2; ++ni) {
          int colbase = wn * 64 + ni * 32 + (g & 1) * 16;
          int krow = ks * 16 + 8 * hh + q4;
          unsigned a0 = (unsigned)(size_t)(sB + krow * B_STRIDE + (colbase + 4 * p4) * 2);
          s16x4 lo = tr_read(a0);
          s16x4 hi = tr_read(a0 + 4 * B_STRIDE);
          bfr[ni] = __builtin_shufflevector(lo, hi, 0, 1, 2, 3, 4, 5, 6, 7);
        }
        if (isV) {
#pragma unroll
          for (int mi = 0; mi < 2; ++mi)
#pragma unroll
            for (int ni = 0; ni < 2; ++ni) acc[mi][ni] = MFMA32(af[mi], bfr[ni], acc[mi][ni]);
        } else {
#pragma unroll
          for (int mi = 0; mi < 2; ++mi)
#pragma unroll
            for (int ni = 0; ni < 2; ++ni) acc[mi][ni] = MFMA32(bfr[ni], af[mi], acc[mi][ni]);
        }
      }
    }
#pragma unroll
    for (int mi = 0; mi < 2; ++mi)
#pragma unroll
      for (int ni = 0; ni < 2; ++ni) {
        const int nbase = bn * 128 + wn * 64 + ni * 32;
        const int tbase = bm * 128 + wm * 64 + mi * 32;
        if (!isV) {
          u16* dst = (u16*)(ws + (nbase < 512 ? OFF_Q : OFF_K));
          const int head0 = (nbase & 511) >> 4;
          const int token = tbase + r32;
#pragma unroll
          for (int hb = 0; hb < 2; ++hb) {
            uint4 v;
            v.x = pack2(acc[mi][ni][8 * hb + 0], acc[mi][ni][8 * hb + 1]);
            v.y = pack2(acc[mi][ni][8 * hb + 2], acc[mi][ni][8 * hb + 3]);
            v.z = pack2(acc[mi][ni][8 * hb + 4], acc[mi][ni][8 * hb + 5]);
            v.w = pack2(acc[mi][ni][8 * hb + 6], acc[mi][ni][8 * hb + 7]);
            *(uint4*)(dst + ((size_t)(head0 + hb) * S + token) * DH + 8 * hh) = v;
          }
        } else {
          u16* dst = (u16*)(ws + OFF_V);
          const int n = nbase - 1024 + r32;
          const int head = n >> 4, dh = n & 15;
#pragma unroll
          for (int sb = 0; sb < 2; ++sb) {
            uint4 v;
            v.x = pack2(acc[mi][ni][8 * sb + 0], acc[mi][ni][8 * sb + 1]);
            v.y = pack2(acc[mi][ni][8 * sb + 2], acc[mi][ni][8 * sb + 3]);
            v.z = pack2(acc[mi][ni][8 * sb + 4], acc[mi][ni][8 * sb + 5]);
            v.w = pack2(acc[mi][ni][8 * sb + 6], acc[mi][ni][8 * sb + 7]);
            *(uint4*)(dst + ((size_t)head * DH + dh) * S + tbase + 16 * sb + 8 * hh) = v;
          }
        }
      }
  } else {
    const int pb = bid - K1_GEMM_BLOCKS;
    const int t0 = (pb >> 1) * 64, c0 = (pb & 1) * 256;
    u16* tile = (u16*)smem;
    constexpr int TS = 72;
    {
      const int cx = tid & 31, ty = tid >> 5;
      const int ch = c0 + cx * 8;
      float w[8][4];
#pragma unroll
      for (int e = 0; e < 8; ++e)
#pragma unroll
        for (int j = 0; j < 4; ++j) w[e][j] = bf2f(shift_k[(ch + e) * 4 + j]);
      float kr[11][8];
#pragma unroll
      for (int r = 0; r < 11; ++r) {
        int t = t0 + ty * 8 + r - 3;
        if (t >= 0) {
          uint4 v = *(const uint4*)(kin + (size_t)t * D + ch);
#pragma unroll
          for (int e = 0; e < 8; ++e) kr[r][e] = bf2f(ext8(v, e));
        } else {
#pragma unroll
          for (int e = 0; e < 8; ++e) kr[r][e] = 0.f;
        }
      }
#pragma unroll
      for (int r = 0; r < 8; ++r) {
        int t = t0 + ty * 8 + r;
        uint4 v = *(const uint4*)(vin + (size_t)t * D + ch);
#pragma unroll
        for (int e = 0; e < 8; ++e) {
          float ksft = 0.f;
          ksft = rbf(ksft + rbf(kr[r + 0][e] * w[e][3]));
          ksft = rbf(ksft + rbf(kr[r + 1][e] * w[e][2]));
          ksft = rbf(ksft + rbf(kr[r + 2][e] * w[e][1]));
          ksft = rbf(ksft + rbf(kr[r + 3][e] * w[e][0]));
          tile[(cx * 8 + e) * TS + ty * 8 + r] = f2bf(ksft * bf2f(ext8(v, e)));
        }
      }
    }
    __syncthreads();
    {
      u16* kvT = (u16*)(ws + OFF_KVT);
      for (int c = tid; c < 256 * 8; c += 256) {
        int ch = c >> 3, seg = c & 7;
        uint4 v = *(const uint4*)(tile + ch * TS + seg * 8);
        *(uint4*)(kvT + (size_t)(c0 + ch) * S + t0 + seg * 8) = v;
      }
    }
    if ((pb & 1) == 0 || true) {
      float* g = (float*)(ws + OFF_G);
      for (int tt = wid; tt < 32; tt += 4) {
        int t = pb * 32 + tt;
        uint4 xv = *(const uint4*)(x + (size_t)t * D + lane * 8);
        uint4 gv = *(const uint4*)(gate_w + lane * 8);
        float s = 0.f;
#pragma unroll
        for (int e = 0; e < 8; ++e) s += rbf(bf2f(ext8(xv, e)) * bf2f(ext8(gv, e)));
#pragma unroll
        for (int off = 32; off > 0; off >>= 1) s += __shfl_xor(s, off);
        if (lane == 0) {
          float sb = rbf(s);
          g[t] = rbf(1.f / (1.f + __expf(-sb)));
        }
      }
    }
  }
}

constexpr int K2_ATTN_BLOCKS = 256;
constexpr int K2_CONV_BLOCKS = 512;
constexpr int KCH = 256;
constexpr int VROW = KCH * 2 + 16;
constexpr int ATTN_LDS = 2 * (KCH * 32) + 2 * (16 * VROW) + 64;
constexpr int CS = 2208;
constexpr int CONV_LDS = 4 * CS * 2 + 128 * 40 * 2;

__global__ __launch_bounds__(512) void k2_attn_conv(const u16* __restrict__ ssm_k, const u16* __restrict__ ssm_D,
                                                   char* __restrict__ ws) {
  __shared__ __attribute__((aligned(16))) char smem[(ATTN_LDS > CONV_LDS ? ATTN_LDS : CONV_LDS)];
  const int tid = threadIdx.x;
  const int lane = tid & 63, wid = tid >> 6;
  const int r32 = lane & 31, hh = lane >> 5;
  const int bid = blockIdx.x;
  if (bid < K2_ATTN_BLOCKS) {
    const int hd = bid >> 3, qb = bid & 7;
    const u16* Qw = (const u16*)(ws + OFF_Q) + (size_t)hd * S * DH;
    const u16* Kw = (const u16*)(ws + OFF_K) + (size_t)hd * S * DH;
    const u16* Vt = (const u16*)(ws + OFF_V) + (size_t)hd * DH * S;
    char* sK = smem;
    char* sV = smem + 2 * KCH * 32;
    char* sC = smem + 2 * KCH * 32 + 2 * 16 * VROW;
    if (tid < 8) ((u16*)sC)[tid] = 0x3F80;
    if (tid >= 8 && tid < 32) ((u16*)sC)[tid] = 0;
    const int q0 = qb * 256 + wid * 32;
    const bf16x8 qf = *(const bf16x8*)(Qw + (size_t)(q0 + r32) * DH + 8 * hh);
    f32x16 acc;
#pragma unroll
    for (int e = 0; e < 16; ++e) acc[e] = 0.f;
    float m_run = -1e30f;
    const float cs = 0.25f * 1.4426950408889634f;
    const int vrow = r32;
    const unsigned v_lane_base = (vrow < 16) ? (unsigned)(vrow * VROW + 16 * hh) : 0u;
    const unsigned v_lane_step = (vrow < 16) ? 64u : 0u;
    const unsigned v_const = (unsigned)(size_t)sC + ((vrow == 16) ? 0u : 32u);

    uint4 stK, stV;
    {
      stK = *(const uint4*)(Kw + (size_t)tid * 8);
      int vr = tid >> 5, vs = tid & 31;
      stV = *(const uint4*)(Vt + (size_t)vr * S + vs * 8);
      *(uint4*)(sK + tid * 16) = stK;
      *(uint4*)(sV + vr * VROW + vs * 16) = stV;
    }
    __syncthreads();
    for (int ch = 0; ch < S / KCH; ++ch) {
      const int buf = ch & 1;
      if (ch + 1 < S / KCH) {
        stK = *(const uint4*)(Kw + (size_t)(ch + 1) * KCH * DH + (size_t)tid * 8);
        int vr = tid >> 5, vs = tid & 31;
        stV = *(const uint4*)(Vt + (size_t)vr * S + (ch + 1) * KCH + vs * 8);
      }
      const char* kb = sK + buf * KCH * 32;
      const unsigned vb = (unsigned)(size_t)(sV + buf * 16 * VROW);
#pragma unroll 2
      for (int T = 0; T < KCH / 32; ++T) {
        bf16x8 kf = *(const bf16x8*)(kb + (T * 32 + r32) * 32 + 16 * hh);
        f32x16 zero;
#pragma unroll
        for (int e = 0; e < 16; ++e) zero[e] = 0.f;
        f32x16 s = MFMA32(kf, qf, zero);
        float mt = fmaxf(fmaxf(s[0], s[1]), s[2]);
#pragma unroll
        for (int e = 3; e < 15; e += 2) mt = fmaxf(fmaxf(mt, s[e]), s[e + 1]);
        mt = fmaxf(mt, s[15]);
        mt = fmaxf(mt, __shfl_xor(mt, 32));
        float m_new = fmaxf(m_run, mt);
        if (__any(m_new > m_run)) {
          float alpha = exp2f((m_run - m_new) * cs);
#pragma unroll
          for (int e = 0; e < 16; ++e) acc[e] *= alpha;
          m_run = m_new;
        }
        const float mc = m_run * cs;
        float p[16];
#pragma unroll
        for (int e = 0; e < 16; ++e) p[e] = exp2f(fmaf(s[e], cs, -mc));
        u32x4 pk0, pk1;
        pk0[0] = pack2(p[0], p[1]); pk0[1] = pack2(p[2], p[3]); pk0[2] = pack2(p[4], p[5]); pk0[3] = pack2(p[6], p[7]);
        pk1[0] = pack2(p[8], p[9]); pk1[1] = pack2(p[10], p[11]); pk1[2] = pack2(p[12], p[13]); pk1[3] = pack2(p[14], p[15]);
        const unsigned va0 = (vrow < 16) ? (vb + v_lane_base + T * v_lane_step) : v_const;
        const unsigned va1 = (vrow < 16) ? (va0 + 32) : v_const;
        bf16x8 vf0 = *(lds_bf16x8_ptr)(size_t)(va0);
        bf16x8 vf1 = *(lds_bf16x8_ptr)(size_t)(va1);
        acc = MFMA32(vf0, __builtin_bit_cast(bf16x8, pk0), acc);
        acc = MFMA32(vf1, __builtin_bit_cast(bf16x8, pk1), acc);
      }
      if (ch + 1 < S / KCH) {
        char* nk = sK + (buf ^ 1) * KCH * 32;
        char* nv = sV + (buf ^ 1) * 16 * VROW;
        *(uint4*)(nk + tid * 16) = stK;
        int vr = tid >> 5, vs = tid & 31;
        *(uint4*)(nv + vr * VROW + vs * 16) = stV;
      }
      __syncthreads();
    }
    float l = __shfl(acc[8], r32);
    float inv = 1.f / l;
    u16* o = (u16*)(ws + OFF_O);
    uint2 w0, w1;
    w0.x = pack2(acc[0] * inv, acc[1] * inv); w0.y = pack2(acc[2] * inv, acc[3] * inv);
    w1.x = pack2(acc[4] * inv, acc[5] * inv); w1.y = pack2(acc[6] * inv, acc[7] * inv);
    *(uint2*)(o + (size_t)(q0 + r32) * D + hd * DH + 4 * hh) = w0;
    *(uint2*)(o + (size_t)(q0 + r32) * D + hd * DH + 8 + 4 * hh) = w1;
  } else {
    const int d = bid - K2_ATTN_BLOCKS;
    u16* hs = (u16*)smem;
    u16* kvp = hs + 4 * CS;
    const u16* hg = ssm_k + (size_t)d * S;
    const u16* kvT = (const u16*)(ws + OFF_KVT) + (size_t)d * S;
    for (int i = tid; i < 4 * CS + 128 * 40; i += 512) hs[i] = 0;
    __syncthreads();
    if (tid < 256) {
      uint4 v = *(const uint4*)(hg + tid * 8);
#pragma unroll
      for (int e = 0; e < 8; ++e) {
        int idx = tid * 8 + e;
        int m = 2047 - idx;
#pragma unroll
        for (int r = 0; r < 4; ++r) hs[r * CS + m + r] = ext8(v, e);
      }
      uint4 kv4 = *(const uint4*)(kvT + tid * 8);
      int blk = tid >> 2, j = (tid & 3) * 8;
      *(uint4*)(kvp + (64 + blk) * 40 + j) = kv4;
    }
    __syncthreads();
    f32x16 acc0, acc1;
#pragma unroll
    for (int e = 0; e < 16; ++e) { acc0[e] = 0.f; acc1[e] = 0.f; }
    if (wid < 4) {
      for (int dl = wid; dl < 64; dl += 4) {
#pragma unroll
        for (int ks = 0; ks < 2; ++ks) {
          int base = 32 * dl + r32 - 16 * ks - 8 * hh;
          int st = 2047 - base;
          int r = (-st) & 3;
          const u16* ap = hs + r * CS + st + r;
          uint2 a0 = *(const uint2*)(ap);
          uint2 a1 = *(const uint2*)(ap + 4);
          u32x4 av; av[0] = a0.x; av[1] = a0.y; av[2] = a1.x; av[3] = a1.y;
          bf16x8 af = __builtin_bit_cast(bf16x8, av);
          bf16x8 b1 = *(const bf16x8*)(kvp + (64 + 32 + r32 - dl) * 40 + 16 * ks + 8 * hh);
          acc1 = MFMA32(af, b1, acc1);
          if (dl < 32) {
            bf16x8 b0 = *(const bf16x8*)(kvp + (64 + r32 - dl) * 40 + 16 * ks + 8 * hh);
            acc0 = MFMA32(af, b0, acc0);
          }
        }
      }
    }
    __syncthreads();
    float* red = (float*)smem;
    if (wid >= 1 && wid < 4) {
#pragma unroll
      for (int e = 0; e < 16; ++e) {
        red[((wid - 1) * 32 + e) * 64 + lane] = acc0[e];
        red[((wid - 1) * 32 + 16 + e) * 64 + lane] = acc1[e];
      }
    }
    __syncthreads();
    if (wid == 0) {
#pragma unroll
      for (int w = 0; w < 3; ++w)
#pragma unroll
        for (int e = 0; e < 16; ++e) {
          acc0[e] += red[(w * 32 + e) * 64 + lane];
          acc1[e] += red[(w * 32 + 16 + e) * 64 + lane];
        }
      const float Dd = bf2f(ssm_D[d]);
      u16* zT = (u16*)(ws + OFF_ZT) + (size_t)d * S;
#pragma unroll
      for (int nt = 0; nt < 2; ++nt) {
#pragma unroll
        for (int g = 0; g < 4; ++g) {
          int t = 32 * (32 * nt + r32) + 8 * g + 4 * hh;
          uint2 kvv = *(const uint2*)(kvT + t);
          u16 zz[4];
#pragma unroll
          for (int j = 0; j < 4; ++j) {
            float y = nt ? acc1[4 * g + j] : acc0[4 * g + j];
            zz[j] = f2bf(rbf(y) + rbf(bf2f(ext4(kvv, j)) * Dd));
          }
          uint2 o;
          o.x = (unsigned)zz[0] | ((unsigned)zz[1] << 16);
          o.y = (unsigned)zz[2] | ((unsigned)zz[3] << 16);
          *(uint2*)(zT + t) = o;
        }
      }
    }
  }
}

__global__ __launch_bounds__(256) void k3_out(const u16* __restrict__ qin, const u16* __restrict__ wo,
                                             const char* __restrict__ ws, u16* __restrict__ out) {
  __shared__ __attribute__((aligned(16))) char smem[64 * A_STRIDE + 64 * 144];
  const int tid = threadIdx.x;
  const int lane = tid & 63, wid = tid >> 6;
  const int r32 = lane & 31, hh = lane >> 5;
  const int bm = blockIdx.x >> 3, bn = blockIdx.x & 7;
  const u16* o = (const u16*)(ws + OFF_O);
  char* sA = smem;
  char* sB = smem + 64 * A_STRIDE;
  constexpr int BS3 = 144;
  const int wm = wid >> 1, wn = wid & 1;
  f32x16 acc;
#pragma unroll
  for (int e = 0; e < 16; ++e) acc[e] = 0.f;
  for (int k0 = 0; k0 < D; k0 += 64) {
    u32x4 ra[2], rb[2];
#pragma unroll
    for (int i = 0; i < 2; ++i) {
      int c = tid + 256 * i;
      int row = c >> 3, kc = c & 7;
      ra[i] = *(const u32x4*)(o + (size_t)(bm * 64 + row) * D + k0 + kc * 8);
      rb[i] = *(const u32x4*)(wo + (size_t)(k0 + row) * D + bn * 64 + kc * 8);
    }
    __syncthreads();
#pragma unroll
    for (int i = 0; i < 2; ++i) {
      int c = tid + 256 * i;
      int row = c >> 3, kc = c & 7;
      *(u32x4*)(sA + row * A_STRIDE + kc * 16) = ra[i];
      *(u32x4*)(sB + row * BS3 + kc * 16) = rb[i];
    }
    __syncthreads();
#pragma unroll
    for (int ks = 0; ks < 4; ++ks) {
      bf16x8 af = *(const bf16x8*)(sA + (wm * 32 + r32) * A_STRIDE + (ks * 16 + 8 * hh) * 2);
      const int i16 = lane & 15, q4 = i16 >> 2, p4 = i16 & 3, g = lane >> 4;
      int colbase = wn * 32 + (g & 1) * 16;
      int krow = ks * 16 + 8 * hh + q4;
      unsigned a0 = (unsigned)(size_t)(sB + krow * BS3 + (colbase + 4 * p4) * 2);
      s16x4 lo = tr_read(a0);
      s16x4 hi = tr_read(a0 + 4 * BS3);
      bf16x8 bfr = __builtin_shufflevector(lo, hi, 0, 1, 2, 3, 4, 5, 6, 7);
      acc = MFMA32(af, bfr, acc);
    }
  }
  const u16* zT = (const u16*)(ws + OFF_ZT);
  const float* g = (const float*)(ws + OFF_G);
  const int n = bn * 64 + wn * 32 + r32;
#pragma unroll
  for (int gq = 0; gq < 4; ++gq) {
    int t = bm * 64 + wm * 32 + 8 * gq + 4 * hh;
    uint2 zv = *(const uint2*)(zT + (size_t)n * S + t);
#pragma unroll
    for (int j = 0; j < 4; ++j) {
      float gg = g[t + j];
      float a = rbf(bf2f(qin[(size_t)(t + j) * D + n]) * bf2f(ext4(zv, j)));
      float b = rbf(acc[4 * gq + j]);
      float r = rbf(rbf(gg * a) + rbf(rbf(1.f - gg) * b));
      out[(size_t)(t + j) * D + n] = f2bf(r);
    }
  }
}

extern "C" void kernel_launch(void* const* d_in, const int* in_sizes, int n_in, void* d_out, int out_size,
                              void* d_ws, size_t ws_size, hipStream_t stream) {
  const u16* A_q = (const u16*)d_in[0];
  const u16* A_k = (const u16*)d_in[1];
  const u16* A_v = (const u16*)d_in[2];
  const u16* A_shift_k = (const u16*)d_in[3];
  const u16* A_ssm_k = (const u16*)d_in[4];
  const u16* A_ssm_D = (const u16*)d_in[5];
  const u16* B_wqkv = (const u16*)d_in[7];
  const u16* B_wo = (const u16*)d_in[8];
  const u16* gate_w = (const u16*)d_in[9];
  char* ws = (char*)d_ws;
  k1_qkv_prep<<<K1_GEMM_BLOCKS + K1_PREP_BLOCKS, 256, 0, stream>>>(A_q, A_k, A_v, A_shift_k, B_wqkv, gate_w, ws);
  k2_attn_conv<<<K2_ATTN_BLOCKS + K2_CONV_BLOCKS, 512, 0, stream>>>(A_ssm_k, A_ssm_D, ws);
  k3_out<<<256, 256, 0, stream>>>(A_q, B_wo, ws, (u16*)d_out);
}
```

```cpp
#include <hip/hip_runtime.h>
#include <hip/hip_bf16.h>
#include <stdint.h>

typedef __attribute__((ext_vector_type(8))) short bf16x8;
typedef __attribute__((ext_vector_type(4))) short s16x4;
typedef __attribute__((ext_vector_type(16))) float f32x16;
typedef __attribute__((ext_vector_type(4))) unsigned u32x4;
typedef unsigned short u16;

#define DI __device__ __forceinline__
#define MFMA32(a, b, c) __builtin_amdgcn_mfma_f32_32x32x16_bf16((a), (b), (c), 0, 0, 0)

constexpr int S = 2048;
constexpr int D = 512;
constexpr int H = 32;
constexpr int DH = 16;

DI u16 f2bf(float x) {
  unsigned u = __float_as_uint(x);
  u += 0x7fffu + ((u >> 16) & 1u);
  return (u16)(u >> 16);
}
DI float bf2f(u16 b) { return __uint_as_float(((unsigned)b) << 16); }
DI float rbf(float x) { return bf2f(f2bf(x)); }

DI unsigned comp4(const uint4& v, int i) { return i == 0 ? v.x : i == 1 ? v.y : i == 2 ? v.z : v.w; }
DI u16 ext8(const uint4& v, int e) { unsigned w = comp4(v, e >> 1); return (u16)((e & 1) ? (w >> 16) : (w & 0xffffu)); }
DI u16 ext4(const uint2& v, int e) { unsigned w = (e >> 1) ? v.y : v.x; return (u16)((e & 1) ? (w >> 16) : (w & 0xffffu)); }
typedef __attribute__((ext_vector_type(2))) __bf16 bf16x2_t;
typedef __attribute__((ext_vector_type(2))) float f32x2;
DI unsigned pack2(float lo, float hi) {
  f32x2 v = {lo, hi};
  bf16x2_t b = __builtin_convertvector(v, bf16x2_t);
  return __builtin_bit_cast(unsigned, b);
}

typedef __attribute__((address_space(3))) s16x4* lds_s16x4_ptr;
typedef __attribute__((address_space(3))) const bf16x8* lds_bf16x8_ptr;
DI s16x4 tr_read(unsigned lds_addr) {
  return __builtin_amdgcn_ds_read_tr16_b64_v4i16((lds_s16x4_ptr)(size_t)lds_addr);
}

constexpr size_t OFF_Q = 0;
constexpr size_t OFF_K = OFF_Q + (size_t)H * S * DH * 2;
constexpr size_t OFF_V = OFF_K + (size_t)H * S * DH * 2;
constexpr size_t OFF_KVT = OFF_V + (size_t)H * S * DH * 2;
constexpr size_t OFF_ZT = OFF_KVT + (size_t)D * S * 2;
constexpr size_t OFF_O = OFF_ZT + (size_t)D * S * 2;
constexpr size_t OFF_G = OFF_O + (size_t)S * D * 2;

constexpr float QSCALE = 0.25f * 1.4426950408889634f;
constexpr float SM_THR = 8.0f;
constexpr int K1_GEMM_BLOCKS = 192;
constexpr int K1_PREP_BLOCKS = 256;
constexpr int A_STRIDE = 144;
constexpr int B_STRIDE = 320;

__global__ __launch_bounds__(256) void k1_qkv_prep(const u16* __restrict__ x, const u16* __restrict__ kin,
                                                  const u16* __restrict__ vin, const u16* __restrict__ shift_k,
                                                  const u16* __restrict__ wqkv, const u16* __restrict__ gate_w,
                                                  char* __restrict__ ws) {
  __shared__ __attribute__((aligned(16))) char smem[2 * (128 * A_STRIDE + 64 * B_STRIDE)];
  const int tid = threadIdx.x;
  const int lane = tid & 63, wid = tid >> 6;
  const int bid = blockIdx.x;
  if (bid < K1_GEMM_BLOCKS) {
    const int bm = bid / 12, bn = bid % 12;
    char* sA = smem;
    char* sB = smem + 128 * A_STRIDE;
    const int wm = wid >> 1, wn = wid & 1;
    const int r32 = lane & 31, hh = lane >> 5;
    f32x16 acc[2][2];
#pragma unroll
    for (int i = 0; i < 2; ++i)
#pragma unroll
      for (int j = 0; j < 2; ++j)
#pragma unroll
        for (int e = 0; e < 16; ++e) acc[i][j][e] = 0.f;
    const bool isV = (bn >= 8);
    constexpr int BUFSZ = 128 * A_STRIDE + 64 * B_STRIDE;
    u32x4 ra[4], rb[4];
    auto gload = [&](int k0) {
#pragma unroll
      for (int i = 0; i < 4; ++i) {
        int c = tid + 256 * i;
        int row = c >> 3, kc = c & 7;
        ra[i] = *(const u32x4*)(x + (size_t)(bm * 128 + row) * D + k0 + kc * 8);
        int krow = c >> 4, nc = c & 15;
        rb[i] = *(const u32x4*)(wqkv + (size_t)(k0 + krow) * (3 * D) + bn * 128 + nc * 8);
      }
    };
    auto sstore = [&](int b) {
#pragma unroll
      for (int i = 0; i < 4; ++i) {
        int c = tid + 256 * i;
        int row = c >> 3, kc = c & 7;
        *(u32x4*)(sA + b * BUFSZ + row * A_STRIDE + kc * 16) = ra[i];
        int krow = c >> 4, nc = c & 15;
        *(u32x4*)(sB + b * BUFSZ + krow * B_STRIDE + nc * 16) = rb[i];
      }
    };
    gload(0);
    sstore(0);
    __syncthreads();
    for (int kt = 0; kt < 8; ++kt) {
      const int b = kt & 1;
      if (kt + 1 < 8) gload((kt + 1) * 64);
      const char* cA = sA + b * BUFSZ;
      const char* cB = sB + b * BUFSZ;
#pragma unroll
      for (int ks = 0; ks < 4; ++ks) {
        bf16x8 af[2], bfr[2];
#pragma unroll
        for (int mi = 0; mi < 2; ++mi) {
          int row = wm * 64 + mi * 32 + r32;
          af[mi] = *(const bf16x8*)(cA + row * A_STRIDE + (ks * 16 + 8 * hh) * 2);
        }
        const int i16 = lane & 15, q4 = i16 >> 2, p4 = i16 & 3, g = lane >> 4;
#pragma unroll
        for (int ni = 0; ni < 2; ++ni) {
          int colbase = wn * 64 + ni * 32 + (g & 1) * 16;
          int krow = ks * 16 + 8 * hh + q4;
          unsigned a0 = (unsigned)(size_t)(cB + krow * B_STRIDE + (colbase + 4 * p4) * 2);
          s16x4 lo = tr_read(a0);
          s16x4 hi = tr_read(a0 + 4 * B_STRIDE);
          bfr[ni] = __builtin_shufflevector(lo, hi, 0, 1, 2, 3, 4, 5, 6, 7);
        }
        if (isV) {
#pragma unroll
          for (int mi = 0; mi < 2; ++mi)
#pragma unroll
            for (int ni = 0; ni < 2; ++ni) acc[mi][ni] = MFMA32(af[mi], bfr[ni], acc[mi][ni]);
        } else {
#pragma unroll
          for (int mi = 0; mi < 2; ++mi)
#pragma unroll
            for (int ni = 0; ni < 2; ++ni) acc[mi][ni] = MFMA32(bfr[ni], af[mi], acc[mi][ni]);
        }
      }
      if (kt + 1 < 8) sstore(b ^ 1);
      __syncthreads();
    }
#pragma unroll
    for (int mi = 0; mi < 2; ++mi)
#pragma unroll
      for (int ni = 0; ni < 2; ++ni) {
        const int nbase = bn * 128 + wn * 64 + ni * 32;
        const int tbase = bm * 128 + wm * 64 + mi * 32;
        if (!isV) {
          u16* dst = (u16*)(ws + (nbase < 512 ? OFF_Q : OFF_K));
          const int head0 = (nbase & 511) >> 4;
          const int token = tbase + r32;
          const float qs = (nbase < 512) ? QSCALE : 1.f;
#pragma unroll
          for (int hb = 0; hb < 2; ++hb) {
            uint4 v;
            v.x = pack2(acc[mi][ni][8 * hb + 0] * qs, acc[mi][ni][8 * hb + 1] * qs);
            v.y = pack2(acc[mi][ni][8 * hb + 2] * qs, acc[mi][ni][8 * hb + 3] * qs);
            v.z = pack2(acc[mi][ni][8 * hb + 4] * qs, acc[mi][ni][8 * hb + 5] * qs);
            v.w = pack2(acc[mi][ni][8 * hb + 6] * qs, acc[mi][ni][8 * hb + 7] * qs);
            *(uint4*)(dst + ((size_t)(head0 + hb) * S + token) * DH + 8 * hh) = v;
          }
        } else {
          u16* dst = (u16*)(ws + OFF_V);
          const int n = nbase - 1024 + r32;
          const int head = n >> 4, dh = n & 15;
#pragma unroll
          for (int sb = 0; sb < 2; ++sb) {
            uint4 v;
            v.x = pack2(acc[mi][ni][8 * sb + 0], acc[mi][ni][8 * sb + 1]);
            v.y = pack2(acc[mi][ni][8 * sb + 2], acc[mi][ni][8 * sb + 3]);
            v.z = pack2(acc[mi][ni][8 * sb + 4], acc[mi][ni][8 * sb + 5]);
            v.w = pack2(acc[mi][ni][8 * sb + 6], acc[mi][ni][8 * sb + 7]);
            *(uint4*)(dst + ((size_t)head * DH + dh) * S + tbase + 16 * sb + 8 * hh) = v;
          }
        }
      }
  } else {
    const int pb = bid - K1_GEMM_BLOCKS;
    const int t0 = (pb >> 3) * 64, c0 = (pb & 7) * 64;
    u16* tile = (u16*)smem;
    constexpr int TS = 72;
    {
      const int cx = tid & 7, ty = tid >> 3;
      const int ch = c0 + cx * 8;
      uint4 wv0 = *(const uint4*)(shift_k + ch * 4);
      uint4 wv1 = *(const uint4*)(shift_k + ch * 4 + 8);
      uint4 wv2 = *(const uint4*)(shift_k + ch * 4 + 16);
      uint4 wv3 = *(const uint4*)(shift_k + ch * 4 + 24);
      uint4 kr[5];
#pragma unroll
      for (int r = 0; r < 5; ++r) {
        int t = t0 + ty * 2 + r - 3;
        if (t >= 0) kr[r] = *(const uint4*)(kin + (size_t)t * D + ch);
        else kr[r] = make_uint4(0u, 0u, 0u, 0u);
      }
#pragma unroll
      for (int r = 0; r < 2; ++r) {
        int t = t0 + ty * 2 + r;
        uint4 v = *(const uint4*)(vin + (size_t)t * D + ch);
#pragma unroll
        for (int e = 0; e < 8; ++e) {
          const uint4& wv = (e < 2) ? wv0 : (e < 4) ? wv1 : (e < 6) ? wv2 : wv3;
          float w0 = bf2f(ext8(wv, (e & 1) * 4 + 0)), w1 = bf2f(ext8(wv, (e & 1) * 4 + 1));
          float w2 = bf2f(ext8(wv, (e & 1) * 4 + 2)), w3 = bf2f(ext8(wv, (e & 1) * 4 + 3));
          float ksft = 0.f;
          ksft = rbf(ksft + rbf(bf2f(ext8(kr[r + 0], e)) * w3));
          ksft = rbf(ksft + rbf(bf2f(ext8(kr[r + 1], e)) * w2));
          ksft = rbf(ksft + rbf(bf2f(ext8(kr[r + 2], e)) * w1));
          ksft = rbf(ksft + rbf(bf2f(ext8(kr[r + 3], e)) * w0));
          tile[(cx * 8 + e) * TS + ty * 2 + r] = f2bf(ksft * bf2f(ext8(v, e)));
        }
      }
    }
    __syncthreads();
    {
      u16* kvT = (u16*)(ws + OFF_KVT);
      for (int c = tid; c < 64 * 8; c += 256) {
        int ch = c >> 3, seg = c & 7;
        uint4 v = *(const uint4*)(tile + ch * TS + seg * 8);
        *(uint4*)(kvT + (size_t)(c0 + ch) * S + t0 + seg * 8) = v;
      }
    }
    {
      float* g = (float*)(ws + OFF_G);
#pragma unroll
      for (int tt = 0; tt < 2; ++tt) {
        int t = pb * 8 + wid * 2 + tt;
        uint4 xv = *(const uint4*)(x + (size_t)t * D + lane * 8);
        uint4 gv = *(const uint4*)(gate_w + lane * 8);
        float s = 0.f;
#pragma unroll
        for (int e = 0; e < 8; ++e) s += rbf(bf2f(ext8(xv, e)) * bf2f(ext8(gv, e)));
#pragma unroll
        for (int off = 32; off > 0; off >>= 1) s += __shfl_xor(s, off);
        if (lane == 0) {
          float sb = rbf(s);
          g[t] = rbf(1.f / (1.f + __expf(-sb)));
        }
      }
    }
  }
}

constexpr int K2_ATTN_BLOCKS = 256;
constexpr int K2_CONV_BLOCKS = 512;
constexpr int KCH = 256;
constexpr int VROW = KCH * 2 + 16;
constexpr int ATTN_LDS = 2 * (KCH * 32) + 2 * (16 * VROW) + 64;
constexpr int CS = 2208;
constexpr int CONV_LDS = 4 * CS * 2 + 128 * 40 * 2;

__global__ __launch_bounds__(512) void k2_attn_conv(const u16* __restrict__ ssm_k, const u16* __restrict__ ssm_D,
                                                    char* __restrict__ ws) {
  __shared__ __attribute__((aligned(16))) char smem[(ATTN_LDS > CONV_LDS ? ATTN_LDS : CONV_LDS)];
  const int tid = threadIdx.x;
  const int lane = tid & 63, wid = tid >> 6;
  const int r32 = lane & 31, hh = lane >> 5;
  const int bid = blockIdx.x;
  if (bid < K2_ATTN_BLOCKS) {
    const int hd = bid >> 3, qb = bid & 7;
    const int qw = wid;
    const u16* Qw = (const u16*)(ws + OFF_Q) + (size_t)hd * S * DH;
    const u16* Kw = (const u16*)(ws + OFF_K) + (size_t)hd * S * DH;
    const u16* Vt = (const u16*)(ws + OFF_V) + (size_t)hd * DH * S;
    char* sK = smem;
    char* sV = smem + 2 * KCH * 32;
    char* sC = smem + 2 * KCH * 32 + 2 * 16 * VROW;
    if (tid < 8) ((u16*)sC)[tid] = 0x3F80;
    if (tid >= 8 && tid < 32) ((u16*)sC)[tid] = 0;
    const int q0 = qb * 256 + qw * 32;
    bf16x8 qf = *(const bf16x8*)(Qw + (size_t)(q0 + r32) * DH + 8 * hh);
    asm volatile("" : "+v"(qf));
    f32x16 acc;
#pragma unroll
    for (int e = 0; e < 16; ++e) acc[e] = 0.f;
    float m_run = 0.f;
    float thr = -3.0e38f;
    f32x16 cneg;
#pragma unroll
    for (int e = 0; e < 16; ++e) cneg[e] = 0.f;
    const int vrow = r32;
    const unsigned v_lane_base = (vrow < 16) ? (unsigned)(vrow * VROW + 16 * hh) : 0u;
    const unsigned v_lane_step = (vrow < 16) ? 64u : 0u;
    const unsigned v_const = (unsigned)(size_t)sC + ((vrow == 16) ? 0u : 32u);

    uint4 st;
    const int t9 = tid & 511;
    const int vr = t9 >> 5, vs = t9 & 31;
    uint4 st2;
    st = *(const uint4*)(Kw + (size_t)t9 * 8); *(uint4*)(sK + t9 * 16) = st;
    st2 = *(const uint4*)(Vt + (size_t)vr * S + vs * 8); *(uint4*)(sV + vr * VROW + vs * 16) = st2;
    __syncthreads();
    for (int ch = 0; ch < S / KCH; ++ch) {
      const int buf = ch & 1;
      if (ch + 1 < S / KCH) {
        st = *(const uint4*)(Kw + (size_t)(ch + 1) * KCH * DH + (size_t)t9 * 8);
        st2 = *(const uint4*)(Vt + (size_t)vr * S + (ch + 1) * KCH + vs * 8);
      }
      const char* kb = sK + buf * KCH * 32;
      const unsigned vb = (unsigned)(size_t)(sV + buf * 16 * VROW);
#pragma unroll 2
      for (int T = 0; T < 8; ++T) {
        bf16x8 kf = *(const bf16x8*)(kb + (T * 32 + r32) * 32 + 16 * hh);
        f32x16 s = MFMA32(kf, qf, cneg);
        const unsigned va0 = (vrow < 16) ? (vb + v_lane_base + T * v_lane_step) : v_const;
        const unsigned va1 = (vrow < 16) ? (va0 + 32) : v_const;
        bf16x8 vf0 = *(lds_bf16x8_ptr)(size_t)(va0);
        bf16x8 vf1 = *(lds_bf16x8_ptr)(size_t)(va1);
        float mt = fmaxf(fmaxf(s[0], s[1]), s[2]);
#pragma unroll
        for (int e = 3; e < 15; e += 2) mt = fmaxf(fmaxf(mt, s[e]), s[e + 1]);
        mt = fmaxf(mt, s[15]);
        if (__any(mt > thr)) {
          auto sw = __builtin_amdgcn_permlane32_swap(__float_as_uint(mt), __float_as_uint(mt), false, false);
          float d = fmaxf(__uint_as_float(sw[0]), __uint_as_float(sw[1]));
          d = (thr < 0.f) ? d : fmaxf(d, 0.f);
          float alpha = __builtin_amdgcn_exp2f(-d);
          alpha = (thr < 0.f) ? 0.f : alpha;
          m_run += d;
#pragma unroll
          for (int e = 0; e < 16; ++e) { acc[e] *= alpha; s[e] -= d; cneg[e] = -m_run; }
          thr = SM_THR;
        }
        float p[16];
#pragma unroll
        for (int e = 0; e < 16; ++e) p[e] = __builtin_amdgcn_exp2f(s[e]);
        u32x4 pk0, pk1;
        pk0[0] = pack2(p[0], p[1]); pk0[1] = pack2(p[2], p[3]); pk0[2] = pack2(p[4], p[5]); pk0[3] = pack2(p[6], p[7]);
        pk1[0] = pack2(p[8], p[9]); pk1[1] = pack2(p[10], p[11]); pk1[2] = pack2(p[12], p[13]); pk1[3] = pack2(p[14], p[15]);
        acc = MFMA32(vf0, __builtin_bit_cast(bf16x8, pk0), acc);
        acc = MFMA32(vf1, __builtin_bit_cast(bf16x8, pk1), acc);
      }
      if (ch + 1 < S / KCH) {
        *(uint4*)(sK + (buf ^ 1) * KCH * 32 + t9 * 16) = st;
        *(uint4*)(sV + (buf ^ 1) * 16 * VROW + vr * VROW + vs * 16) = st2;
      }
      __syncthreads();
    }
    {
      float l = __shfl(acc[8], r32);
      float inv = 1.f / l;
      u16* o = (u16*)(ws + OFF_O);
      uint2 w0, w1;
      w0.x = pack2(acc[0] * inv, acc[1] * inv); w0.y = pack2(acc[2] * inv, acc[3] * inv);
      w1.x = pack2(acc[4] * inv, acc[5] * inv); w1.y = pack2(acc[6] * inv, acc[7] * inv);
      *(uint2*)(o + (size_t)(q0 + r32) * D + hd * DH + 4 * hh) = w0;
      *(uint2*)(o + (size_t)(q0 + r32) * D + hd * DH + 8 + 4 * hh) = w1;
    }
  } else {
    const int d = bid - K2_ATTN_BLOCKS;
    u16* hs = (u16*)smem;
    u16* kvp = hs + 4 * CS;
    const u16* hg = ssm_k + (size_t)d * S;
    const u16* kvT = (const u16*)(ws + OFF_KVT) + (size_t)d * S;
    for (int i = tid; i < 4 * CS + 128 * 40; i += 512) hs[i] = 0;
    __syncthreads();
    if (tid < 256) {
      uint4 v = *(const uint4*)(hg + tid * 8);
#pragma unroll
      for (int e = 0; e < 8; ++e) {
        int idx = tid * 8 + e;
        int m = 2047 - idx;
#pragma unroll
        for (int r = 0; r < 4; ++r) hs[r * CS + m + r] = ext8(v, e);
      }
      uint4 kv4 = *(const uint4*)(kvT + tid * 8);
      int blk = tid >> 2, j = (tid & 3) * 8;
      *(uint4*)(kvp + (64 + blk) * 40 + j) = kv4;
    }
    __syncthreads();
    f32x16 acc0, acc1;
#pragma unroll
    for (int e = 0; e < 16; ++e) { acc0[e] = 0.f; acc1[e] = 0.f; }
    if (wid < 4) {
      for (int dl = wid; dl < 64; dl += 4) {
#pragma unroll
        for (int ks = 0; ks < 2; ++ks) {
          int base = 32 * dl + r32 - 16 * ks - 8 * hh;
          int st = 2047 - base;
          int r = (-st) & 3;
          const u16* ap = hs + r * CS + st + r;
          uint2 a0 = *(const uint2*)(ap);
          uint2 a1 = *(const uint2*)(ap + 4);
          u32x4 av; av[0] = a0.x; av[1] = a0.y; av[2] = a1.x; av[3] = a1.y;
          bf16x8 af = __builtin_bit_cast(bf16x8, av);
          bf16x8 b1 = *(const bf16x8*)(kvp + (64 + 32 + r32 - dl) * 40 + 16 * ks + 8 * hh);
          acc1 = MFMA32(af, b1, acc1);
          if (dl < 32) {
            bf16x8 b0 = *(const bf16x8*)(kvp + (64 + r32 - dl) * 40 + 16 * ks + 8 * hh);
            acc0 = MFMA32(af, b0, acc0);
          }
        }
      }
    }
    __syncthreads();
    float* red = (float*)smem;
    if (wid >= 1 && wid < 4) {
#pragma unroll
      for (int e = 0; e < 16; ++e) {
        red[((wid - 1) * 32 + e) * 64 + lane] = acc0[e];
        red[((wid - 1) * 32 + 16 + e) * 64 + lane] = acc1[e];
      }
    }
    __syncthreads();
    if (wid == 0) {
#pragma unroll
      for (int w = 0; w < 3; ++w)
#pragma unroll
        for (int e = 0; e < 16; ++e) {
          acc0[e] += red[(w * 32 + e) * 64 + lane];
          acc1[e] += red[(w * 32 + 16 + e) * 64 + lane];
        }
      const float Dd = bf2f(ssm_D[d]);
      u16* zT = (u16*)(ws + OFF_ZT) + (size_t)d * S;
#pragma unroll
      for (int nt = 0; nt < 2; ++nt) {
#pragma unroll
        for (int g = 0; g < 4; ++g) {
          int t = 32 * (32 * nt + r32) + 8 * g + 4 * hh;
          uint2 kvv = *(const uint2*)(kvT + t);
          u16 zz[4];
#pragma unroll
          for (int j = 0; j < 4; ++j) {
            float y = nt ? acc1[4 * g + j] : acc0[4 * g + j];
            zz[j] = f2bf(rbf(y) + rbf(bf2f(ext4(kvv, j)) * Dd));
          }
          uint2 o;
          o.x = (unsigned)zz[0] | ((unsigned)zz[1] << 16);
          o.y = (unsigned)zz[2] | ((unsigned)zz[3] << 16);
          *(uint2*)(zT + t) = o;
        }
      }
    }
  }
}

__global__ __launch_bounds__(256) void k3_out(const u16* __restrict__ qin, const u16* __restrict__ wo,
                                             const char* __restrict__ ws, u16* __restrict__ out) {
  constexpr int AS3 = 1040;
  constexpr int BS3 = 144;
  __shared__ __attribute__((aligned(16))) char smem[64 * AS3 + 512 * BS3];
  const int tid = threadIdx.x;
  const int lane = tid & 63, wid = tid >> 6;
  const int r32 = lane & 31, hh = lane >> 5;
  const int bm = blockIdx.x >> 3, bn = blockIdx.x & 7;
  const u16* o = (const u16*)(ws + OFF_O);
  char* sA = smem;
  char* sB = smem + 64 * AS3;
  const int wm = wid >> 1, wn = wid & 1;
  f32x16 acc;
#pragma unroll
  for (int e = 0; e < 16; ++e) acc[e] = 0.f;
  {
    u32x4 ra[16], rb[16];
#pragma unroll
    for (int i = 0; i < 16; ++i) {
      int c = tid + 256 * i;
      int row = c >> 6, kc = c & 63;
      ra[i] = *(const u32x4*)(o + (size_t)(bm * 64 + row) * D + kc * 8);
      int krow = c >> 3, nc = c & 7;
      rb[i] = *(const u32x4*)(wo + (size_t)krow * D + bn * 64 + nc * 8);
    }
#pragma unroll
    for (int i = 0; i < 16; ++i) {
      int c = tid + 256 * i;
      int row = c >> 6, kc = c & 63;
      *(u32x4*)(sA + row * AS3 + kc * 16) = ra[i];
      int krow = c >> 3, nc = c & 7;
      *(u32x4*)(sB + krow * BS3 + nc * 16) = rb[i];
    }
  }
  __syncthreads();
  {
#pragma unroll
    for (int ks = 0; ks < 32; ++ks) {
      bf16x8 af = *(const bf16x8*)(sA + (wm * 32 + r32) * AS3 + (ks * 16 + 8 * hh) * 2);
      const int i16 = lane & 15, q4 = i16 >> 2, p4 = i16 & 3, g = lane >> 4;
      int colbase = wn * 32 + (g & 1) * 16;
      int krow = ks * 16 + 8 * hh + q4;
      unsigned a0 = (unsigned)(size_t)(sB + krow * BS3 + (colbase + 4 * p4) * 2);
      s16x4 lo = tr_read(a0);
      s16x4 hi = tr_read(a0 + 4 * BS3);
      bf16x8 bfr = __builtin_shufflevector(lo, hi, 0, 1, 2, 3, 4, 5, 6, 7);
      acc = MFMA32(bfr, af, acc);
    }
  }
  const u16* zT = (const u16*)(ws + OFF_ZT);
  const float* g = (const float*)(ws + OFF_G);
  const int t = bm * 64 + wm * 32 + r32;
  const float gg = g[t];
  const float omg = rbf(1.f - gg);
#pragma unroll
  for (int gq = 0; gq < 4; ++gq) {
    const int n = bn * 64 + wn * 32 + 8 * gq + 4 * hh;
    uint2 qv = *(const uint2*)(qin + (size_t)t * D + n);
    unsigned ov[2];
    u16 r4[4];
#pragma unroll
    for (int j = 0; j < 4; ++j) {
      float z = bf2f(zT[(size_t)(n + j) * S + t]);
      float a = rbf(bf2f(ext4(qv, j)) * z);
      float b = rbf(acc[4 * gq + j]);
      r4[j] = f2bf(rbf(gg * a) + rbf(omg * b));
    }
    uint2 ovv;
    ovv.x = (unsigned)r4[0] | ((unsigned)r4[1] << 16);
    ovv.y = (unsigned)r4[2] | ((unsigned)r4[3] << 16);
    *(uint2*)(out + (size_t)t * D + n) = ovv;
  }
}

extern "C" void kernel_launch(void* const* d_in, const int* in_sizes, int n_in, void* d_out, int out_size,
                              void* d_ws, size_t ws_size, hipStream_t stream) {
  const u16* A_q = (const u16*)d_in[0];
  const u16* A_k = (const u16*)d_in[1];
  const u16* A_v = (const u16*)d_in[2];
  const u16* A_shift_k = (const u16*)d_in[3];
  const u16* A_ssm_k = (const u16*)d_in[4];
  const u16* A_ssm_D = (const u16*)d_in[5];
  const u16* B_wqkv = (const u16*)d_in[7];
  const u16* B_wo = (const u16*)d_in[8];
  const u16* gate_w = (const u16*)d_in[9];
  char* ws = (char*)d_ws;
  k1_qkv_prep<<<K1_GEMM_BLOCKS + K1_PREP_BLOCKS, 256, 0, stream>>>(A_q, A_k, A_v, A_shift_k, B_wqkv, gate_w, ws);
  k2_attn_conv<<<K2_ATTN_BLOCKS + K2_CONV_BLOCKS, 512, 0, stream>>>(A_ssm_k, A_ssm_D, ws);
  k3_out<<<256, 256, 0, stream>>>(A_q, B_wo, ws, (u16*)d_out);
}
```

```cpp
#include <hip/hip_runtime.h>
#include <hip/hip_bf16.h>
#include <stdint.h>
#include <type_traits>

typedef __attribute__((ext_vector_type(8))) short bf16x8;
typedef __attribute__((ext_vector_type(4))) short s16x4;
typedef __attribute__((ext_vector_type(16))) float f32x16;
typedef __attribute__((ext_vector_type(4))) unsigned u32x4;
typedef unsigned short u16;

#define DI __device__ __forceinline__
#define MFMA32(a, b, c) __builtin_amdgcn_mfma_f32_32x32x16_bf16((a), (b), (c), 0, 0, 0)

constexpr int S = 2048;
constexpr int D = 512;
constexpr int H = 32;
constexpr int DH = 16;

DI u16 f2bf(float x) {
  unsigned u = __float_as_uint(x);
  u += 0x7fffu + ((u >> 16) & 1u);
  return (u16)(u >> 16);
}
DI float bf2f(u16 b) { return __uint_as_float(((unsigned)b) << 16); }
DI float rbf(float x) { return bf2f(f2bf(x)); }

DI unsigned comp4(const uint4& v, int i) { return i == 0 ? v.x : i == 1 ? v.y : i == 2 ? v.z : v.w; }
DI u16 ext8(const uint4& v, int e) { unsigned w = comp4(v, e >> 1); return (u16)((e & 1) ? (w >> 16) : (w & 0xffffu)); }
DI u16 ext4(const uint2& v, int e) { unsigned w = (e >> 1) ? v.y : v.x; return (u16)((e & 1) ? (w >> 16) : (w & 0xffffu)); }
typedef __attribute__((ext_vector_type(2))) __bf16 bf16x2_t;
typedef __attribute__((ext_vector_type(2))) float f32x2;
DI unsigned pack2(float lo, float hi) {
  f32x2 v = {lo, hi};
  bf16x2_t b = __builtin_convertvector(v, bf16x2_t);
  return __builtin_bit_cast(unsigned, b);
}

typedef __attribute__((address_space(3))) s16x4* lds_s16x4_ptr;
typedef __attribute__((address_space(3))) const bf16x8* lds_bf16x8_ptr;
DI s16x4 tr_read(unsigned lds_addr) {
  return __builtin_amdgcn_ds_read_tr16_b64_v4i16((lds_s16x4_ptr)(size_t)lds_addr);
}

constexpr size_t OFF_Q = 0;
constexpr size_t OFF_K = OFF_Q + (size_t)H * S * DH * 2;
constexpr size_t OFF_V = OFF_K + (size_t)H * S * DH * 2;
constexpr size_t OFF_KVT = OFF_V + (size_t)H * S * DH * 2;
constexpr size_t OFF_ZT = OFF_KVT + (size_t)D * S * 2;
constexpr size_t OFF_O = OFF_ZT + (size_t)D * S * 2;
constexpr size_t OFF_G = OFF_O + (size_t)S * D * 2;

constexpr float QSCALE = 0.25f * 1.4426950408889634f;
constexpr float SM_THR = 8.0f;
constexpr int K1_GEMM_BLOCKS = 192;
constexpr int K1_PREP_BLOCKS = 256;
constexpr int A_STRIDE = 144;
constexpr int B_STRIDE = 320;

__global__ __launch_bounds__(256) void k1_qkv_prep(const u16* __restrict__ x, const u16* __restrict__ kin,
                                                  const u16* __restrict__ vin, const u16* __restrict__ shift_k,
                                                  const u16* __restrict__ wqkv, const u16* __restrict__ gate_w,
                                                  char* __restrict__ ws) {
  __shared__ __attribute__((aligned(16))) char smem[2 * (128 * A_STRIDE + 64 * B_STRIDE)];
  const int tid = threadIdx.x;
  const int lane = tid & 63, wid = tid >> 6;
  const int bid = blockIdx.x;
  if (bid < K1_GEMM_BLOCKS) {
    const int bm = bid / 12, bn = bid % 12;
    char* sA = smem;
    char* sB = smem + 128 * A_STRIDE;
    const int wm = wid >> 1, wn = wid & 1;
    const int r32 = lane & 31, hh = lane >> 5;
    f32x16 acc[2][2];
#pragma unroll
    for (int i = 0; i < 2; ++i)
#pragma unroll
      for (int j = 0; j < 2; ++j)
#pragma unroll
        for (int e = 0; e < 16; ++e) acc[i][j][e] = 0.f;
    const bool isV = (bn >= 8);
    constexpr int BUFSZ = 128 * A_STRIDE + 64 * B_STRIDE;
    u32x4 ra[4], rb[4];
    auto gload = [&](int k0) {
#pragma unroll
      for (int i = 0; i < 4; ++i) {
        int c = tid + 256 * i;
        int row = c >> 3, kc = c & 7;
        ra[i] = *(const u32x4*)(x + (size_t)(bm * 128 + row) * D + k0 + kc * 8);
        int krow = c >> 4, nc = c & 15;
        rb[i] = *(const u32x4*)(wqkv + (size_t)(k0 + krow) * (3 * D) + bn * 128 + nc * 8);
      }
    };
    auto sstore = [&](int b) {
#pragma unroll
      for (int i = 0; i < 4; ++i) {
        int c = tid + 256 * i;
        int row = c >> 3, kc = c & 7;
        *(u32x4*)(sA + b * BUFSZ + row * A_STRIDE + kc * 16) = ra[i];
        int krow = c >> 4, nc = c & 15;
        *(u32x4*)(sB + b * BUFSZ + krow * B_STRIDE + nc * 16) = rb[i];
      }
    };
    gload(0);
    sstore(0);
    __syncthreads();
    auto mainloop = [&](auto vtag) {
    for (int kt = 0; kt < 8; ++kt) {
      const int b = kt & 1;
      if (kt + 1 < 8) gload((kt + 1) * 64);
      const char* cA = sA + b * BUFSZ;
      const char* cB = sB + b * BUFSZ;
#pragma unroll
      for (int ks = 0; ks < 4; ++ks) {
        bf16x8 af[2], bfr[2];
#pragma unroll
        for (int mi = 0; mi < 2; ++mi) {
          int row = wm * 64 + mi * 32 + r32;
          af[mi] = *(const bf16x8*)(cA + row * A_STRIDE + (ks * 16 + 8 * hh) * 2);
        }
        const int i16 = lane & 15, q4 = i16 >> 2, p4 = i16 & 3, g = lane >> 4;
#pragma unroll
        for (int ni = 0; ni < 2; ++ni) {
          int colbase = wn * 64 + ni * 32 + (g & 1) * 16;
          int krow = ks * 16 + 8 * hh + q4;
          unsigned a0 = (unsigned)(size_t)(cB + krow * B_STRIDE + (colbase + 4 * p4) * 2);
          s16x4 lo = tr_read(a0);
          s16x4 hi = tr_read(a0 + 4 * B_STRIDE);
          bfr[ni] = __builtin_shufflevector(lo, hi, 0, 1, 2, 3, 4, 5, 6, 7);
        }
        if constexpr (decltype(vtag)::value) {
#pragma unroll
          for (int mi = 0; mi < 2; ++mi)
#pragma unroll
            for (int ni = 0; ni < 2; ++ni) acc[mi][ni] = MFMA32(af[mi], bfr[ni], acc[mi][ni]);
        } else {
#pragma unroll
          for (int mi = 0; mi < 2; ++mi)
#pragma unroll
            for (int ni = 0; ni < 2; ++ni) acc[mi][ni] = MFMA32(bfr[ni], af[mi], acc[mi][ni]);
        }
      }
      if (kt + 1 < 8) sstore(b ^ 1);
      __syncthreads();
    }
    };
    if (isV) mainloop(std::true_type{}); else mainloop(std::false_type{});
#pragma unroll
    for (int mi = 0; mi < 2; ++mi)
#pragma unroll
      for (int ni = 0; ni < 2; ++ni) {
        const int nbase = bn * 128 + wn * 64 + ni * 32;
        const int tbase = bm * 128 + wm * 64 + mi * 32;
        if (!isV) {
          u16* dst = (u16*)(ws + (nbase < 512 ? OFF_Q : OFF_K));
          const int head0 = (nbase & 511) >> 4;
          const int token = tbase + r32;
          const float qs = (nbase < 512) ? QSCALE : 1.f;
#pragma unroll
          for (int hb = 0; hb < 2; ++hb) {
            uint4 v;
            v.x = pack2(acc[mi][ni][8 * hb + 0] * qs, acc[mi][ni][8 * hb + 1] * qs);
            v.y = pack2(acc[mi][ni][8 * hb + 2] * qs, acc[mi][ni][8 * hb + 3] * qs);
            v.z = pack2(acc[mi][ni][8 * hb + 4] * qs, acc[mi][ni][8 * hb + 5] * qs);
            v.w = pack2(acc[mi][ni][8 * hb + 6] * qs, acc[mi][ni][8 * hb + 7] * qs);
            *(uint4*)(dst + ((size_t)(head0 + hb) * S + token) * DH + 8 * hh) = v;
          }
        } else {
          u16* dst = (u16*)(ws + OFF_V);
          const int n = nbase - 1024 + r32;
          const int head = n >> 4, dh = n & 15;
#pragma unroll
          for (int sb = 0; sb < 2; ++sb) {
            uint4 v;
            v.x = pack2(acc[mi][ni][8 * sb + 0], acc[mi][ni][8 * sb + 1]);
            v.y = pack2(acc[mi][ni][8 * sb + 2], acc[mi][ni][8 * sb + 3]);
            v.z = pack2(acc[mi][ni][8 * sb + 4], acc[mi][ni][8 * sb + 5]);
            v.w = pack2(acc[mi][ni][8 * sb + 6], acc[mi][ni][8 * sb + 7]);
            *(uint4*)(dst + ((size_t)head * DH + dh) * S + tbase + 16 * sb + 8 * hh) = v;
          }
        }
      }
  } else {
    const int pb = bid - K1_GEMM_BLOCKS;
    const int t0 = (pb >> 3) * 64, c0 = (pb & 7) * 64;
    u16* tile = (u16*)smem;
    constexpr int TS = 72;
    {
      const int cx = tid & 7, ty = tid >> 3;
      const int ch = c0 + cx * 8;
      uint4 wv0 = *(const uint4*)(shift_k + ch * 4);
      uint4 wv1 = *(const uint4*)(shift_k + ch * 4 + 8);
      uint4 wv2 = *(const uint4*)(shift_k + ch * 4 + 16);
      uint4 wv3 = *(const uint4*)(shift_k + ch * 4 + 24);
      uint4 kr[5];
#pragma unroll
      for (int r = 0; r < 5; ++r) {
        int t = t0 + ty * 2 + r - 3;
        if (t >= 0) kr[r] = *(const uint4*)(kin + (size_t)t * D + ch);
        else kr[r] = make_uint4(0u, 0u, 0u, 0u);
      }
#pragma unroll
      for (int r = 0; r < 2; ++r) {
        int t = t0 + ty * 2 + r;
        uint4 v = *(const uint4*)(vin + (size_t)t * D + ch);
#pragma unroll
        for (int e = 0; e < 8; ++e) {
          const uint4& wv = (e < 2) ? wv0 : (e < 4) ? wv1 : (e < 6) ? wv2 : wv3;
          float w0 = bf2f(ext8(wv, (e & 1) * 4 + 0)), w1 = bf2f(ext8(wv, (e & 1) * 4 + 1));
          float w2 = bf2f(ext8(wv, (e & 1) * 4 + 2)), w3 = bf2f(ext8(wv, (e & 1) * 4 + 3));
          float ksft = 0.f;
          ksft = rbf(ksft + rbf(bf2f(ext8(kr[r + 0], e)) * w3));
          ksft = rbf(ksft + rbf(bf2f(ext8(kr[r + 1], e)) * w2));
          ksft = rbf(ksft + rbf(bf2f(ext8(kr[r + 2], e)) * w1));
          ksft = rbf(ksft + rbf(bf2f(ext8(kr[r + 3], e)) * w0));
          tile[(cx * 8 + e) * TS + ty * 2 + r] = f2bf(ksft * bf2f(ext8(v, e)));
        }
      }
    }
    __syncthreads();
    {
      u16* kvT = (u16*)(ws + OFF_KVT);
      for (int c = tid; c < 64 * 8; c += 256) {
        int ch = c >> 3, seg = c & 7;
        uint4 v = *(const uint4*)(tile + ch * TS + seg * 8);
        *(uint4*)(kvT + (size_t)(c0 + ch) * S + t0 + seg * 8) = v;
      }
    }
    {
      float* g = (float*)(ws + OFF_G);
#pragma unroll
      for (int tt = 0; tt < 2; ++tt) {
        int t = pb * 8 + wid * 2 + tt;
        uint4 xv = *(const uint4*)(x + (size_t)t * D + lane * 8);
        uint4 gv = *(const uint4*)(gate_w + lane * 8);
        float s = 0.f;
#pragma unroll
        for (int e = 0; e < 8; ++e) s += rbf(bf2f(ext8(xv, e)) * bf2f(ext8(gv, e)));
#pragma unroll
        for (int off = 32; off > 0; off >>= 1) s += __shfl_xor(s, off);
        if (lane == 0) {
          float sb = rbf(s);
          g[t] = rbf(1.f / (1.f + __expf(-sb)));
        }
      }
    }
  }
}

constexpr int K2_ATTN_BLOCKS = 256;
constexpr int K2_CONV_BLOCKS = 512;
constexpr int KCH = 256;
constexpr int VROW = KCH * 2 + 16;
constexpr int ATTN_LDS = 2 * (KCH * 32) + 2 * (16 * VROW) + 64;
constexpr int CS = 2208;
constexpr int CONV_LDS = 4 * CS * 2 + 128 * 40 * 2;

__global__ __launch_bounds__(512) void k2_attn_conv(const u16* __restrict__ ssm_k, const u16* __restrict__ ssm_D,
                                                    char* __restrict__ ws) {
  __shared__ __attribute__((aligned(16))) char smem[(ATTN_LDS > CONV_LDS ? ATTN_LDS : CONV_LDS)];
  const int tid = threadIdx.x;
  const int lane = tid & 63, wid = tid >> 6;
  const int r32 = lane & 31, hh = lane >> 5;
  const int bid = blockIdx.x;
  if (bid < K2_ATTN_BLOCKS) {
    const int hd = bid >> 3, qb = bid & 7;
    const int qw = wid;
    const u16* Qw = (const u16*)(ws + OFF_Q) + (size_t)hd * S * DH;
    const u16* Kw = (const u16*)(ws + OFF_K) + (size_t)hd * S * DH;
    const u16* Vt = (const u16*)(ws + OFF_V) + (size_t)hd * DH * S;
    char* sK = smem;
    char* sV = smem + 2 * KCH * 32;
    char* sC = smem + 2 * KCH * 32 + 2 * 16 * VROW;
    if (tid < 8) ((u16*)sC)[tid] = 0x3F80;
    if (tid >= 8 && tid < 32) ((u16*)sC)[tid] = 0;
    const int q0 = qb * 256 + qw * 32;
    bf16x8 qf = *(const bf16x8*)(Qw + (size_t)(q0 + r32) * DH + 8 * hh);
    asm volatile("" : "+v"(qf));
    f32x16 acc;
#pragma unroll
    for (int e = 0; e < 16; ++e) acc[e] = 0.f;
    float m_run = 0.f;
    float thr = -3.0e38f;
    f32x16 cneg;
#pragma unroll
    for (int e = 0; e < 16; ++e) cneg[e] = 0.f;
    const int vrow = r32;
    const unsigned v_lane_base = (vrow < 16) ? (unsigned)(vrow * VROW + 16 * hh) : 0u;
    const unsigned v_lane_step = (vrow < 16) ? 64u : 0u;
    const unsigned v_const = (unsigned)(size_t)sC + ((vrow == 16) ? 0u : 32u);

    uint4 st;
    const int t9 = tid & 511;
    const int vr = t9 >> 5, vs = t9 & 31;
    uint4 st2;
    st = *(const uint4*)(Kw + (size_t)t9 * 8); *(uint4*)(sK + t9 * 16) = st;
    st2 = *(const uint4*)(Vt + (size_t)vr * S + vs * 8); *(uint4*)(sV + vr * VROW + vs * 16) = st2;
    __syncthreads();
    for (int ch = 0; ch < S / KCH; ++ch) {
      const int buf = ch & 1;
      if (ch + 1 < S / KCH) {
        st = *(const uint4*)(Kw + (size_t)(ch + 1) * KCH * DH + (size_t)t9 * 8);
        st2 = *(const uint4*)(Vt + (size_t)vr * S + (ch + 1) * KCH + vs * 8);
      }
      const char* kb = sK + buf * KCH * 32;
      const unsigned vb = (unsigned)(size_t)(sV + buf * 16 * VROW);
#pragma unroll 2
      for (int T = 0; T < 8; ++T) {
        bf16x8 kf = *(const bf16x8*)(kb + (T * 32 + r32) * 32 + 16 * hh);
        f32x16 s = MFMA32(kf, qf, cneg);
        const unsigned va0 = (vrow < 16) ? (vb + v_lane_base + T * v_lane_step) : v_const;
        const unsigned va1 = (vrow < 16) ? (va0 + 32) : v_const;
        bf16x8 vf0 = *(lds_bf16x8_ptr)(size_t)(va0);
        bf16x8 vf1 = *(lds_bf16x8_ptr)(size_t)(va1);
        float mt = fmaxf(fmaxf(s[0], s[1]), s[2]);
#pragma unroll
        for (int e = 3; e < 15; e += 2) mt = fmaxf(fmaxf(mt, s[e]), s[e + 1]);
        mt = fmaxf(mt, s[15]);
        if (__any(mt > thr)) {
          auto sw = __builtin_amdgcn_permlane32_swap(__float_as_uint(mt), __float_as_uint(mt), false, false);
          float d = fmaxf(__uint_as_float(sw[0]), __uint_as_float(sw[1]));
          d = (thr < 0.f) ? d : fmaxf(d, 0.f);
          float alpha = __builtin_amdgcn_exp2f(-d);
          alpha = (thr < 0.f) ? 0.f : alpha;
          m_run += d;
#pragma unroll
          for (int e = 0; e < 16; ++e) { acc[e] *= alpha; s[e] -= d; cneg[e] = -m_run; }
          thr = SM_THR;
        }
        float p[16];
#pragma unroll
        for (int e = 0; e < 16; ++e) p[e] = __builtin_amdgcn_exp2f(s[e]);
        u32x4 pk0, pk1;
        pk0[0] = pack2(p[0], p[1]); pk0[1] = pack2(p[2], p[3]); pk0[2] = pack2(p[4], p[5]); pk0[3] = pack2(p[6], p[7]);
        pk1[0] = pack2(p[8], p[9]); pk1[1] = pack2(p[10], p[11]); pk1[2] = pack2(p[12], p[13]); pk1[3] = pack2(p[14], p[15]);
        acc = MFMA32(vf0, __builtin_bit_cast(bf16x8, pk0), acc);
        acc = MFMA32(vf1, __builtin_bit_cast(bf16x8, pk1), acc);
      }
      if (ch + 1 < S / KCH) {
        *(uint4*)(sK + (buf ^ 1) * KCH * 32 + t9 * 16) = st;
        *(uint4*)(sV + (buf ^ 1) * 16 * VROW + vr * VROW + vs * 16) = st2;
      }
      __syncthreads();
    }
    {
      float l = __shfl(acc[8], r32);
      float inv = 1.f / l;
      u16* o = (u16*)(ws + OFF_O);
      uint2 w0, w1;
      w0.x = pack2(acc[0] * inv, acc[1] * inv); w0.y = pack2(acc[2] * inv, acc[3] * inv);
      w1.x = pack2(acc[4] * inv, acc[5] * inv); w1.y = pack2(acc[6] * inv, acc[7] * inv);
      *(uint2*)(o + (size_t)(q0 + r32) * D + hd * DH + 4 * hh) = w0;
      *(uint2*)(o + (size_t)(q0 + r32) * D + hd * DH + 8 + 4 * hh) = w1;
    }
  } else {
    const int d = bid - K2_ATTN_BLOCKS;
    u16* hs = (u16*)smem;
    u16* kvp = hs + 4 * CS;
    const u16* hg = ssm_k + (size_t)d * S;
    const u16* kvT = (const u16*)(ws + OFF_KVT) + (size_t)d * S;
    for (int i = tid; i < 4 * CS + 128 * 40; i += 512) hs[i] = 0;
    __syncthreads();
    if (tid < 256) {
      uint4 v = *(const uint4*)(hg + tid * 8);
#pragma unroll
      for (int e = 0; e < 8; ++e) {
        int idx = tid * 8 + e;
        int m = 2047 - idx;
#pragma unroll
        for (int r = 0; r < 4; ++r) hs[r * CS + m + r] = ext8(v, e);
      }
      uint4 kv4 = *(const uint4*)(kvT + tid * 8);
      int blk = tid >> 2, j = (tid & 3) * 8;
      *(uint4*)(kvp + (64 + blk) * 40 + j) = kv4;
    }
    __syncthreads();
    f32x16 acc0, acc1;
#pragma unroll
    for (int e = 0; e < 16; ++e) { acc0[e] = 0.f; acc1[e] = 0.f; }
    if (wid < 4) {
      for (int dl = wid; dl < 64; dl += 4) {
#pragma unroll
        for (int ks = 0; ks < 2; ++ks) {
          int base = 32 * dl + r32 - 16 * ks - 8 * hh;
          int st = 2047 - base;
          int r = (-st) & 3;
          const u16* ap = hs + r * CS + st + r;
          uint2 a0 = *(const uint2*)(ap);
          uint2 a1 = *(const uint2*)(ap + 4);
          u32x4 av; av[0] = a0.x; av[1] = a0.y; av[2] = a1.x; av[3] = a1.y;
          bf16x8 af = __builtin_bit_cast(bf16x8, av);
          bf16x8 b1 = *(const bf16x8*)(kvp + (64 + 32 + r32 - dl) * 40 + 16 * ks + 8 * hh);
          acc1 = MFMA32(af, b1, acc1);
          if (dl < 32) {
            bf16x8 b0 = *(const bf16x8*)(kvp + (64 + r32 - dl) * 40 + 16 * ks + 8 * hh);
            acc0 = MFMA32(af, b0, acc0);
          }
        }
      }
    }
    __syncthreads();
    float* red = (float*)smem;
    if (wid >= 1 && wid < 4) {
#pragma unroll
      for (int e = 0; e < 16; ++e) {
        red[((wid - 1) * 32 + e) * 64 + lane] = acc0[e];
        red[((wid - 1) * 32 + 16 + e) * 64 + lane] = acc1[e];
      }
    }
    __syncthreads();
    if (wid == 0) {
#pragma unroll
      for (int w = 0; w < 3; ++w)
#pragma unroll
        for (int e = 0; e < 16; ++e) {
          acc0[e] += red[(w * 32 + e) * 64 + lane];
          acc1[e] += red[(w * 32 + 16 + e) * 64 + lane];
        }
      const float Dd = bf2f(ssm_D[d]);
      u16* zT = (u16*)(ws + OFF_ZT) + (size_t)d * S;
#pragma unroll
      for (int nt = 0; nt < 2; ++nt) {
#pragma unroll
        for (int g = 0; g < 4; ++g) {
          int t = 32 * (32 * nt + r32) + 8 * g + 4 * hh;
          uint2 kvv = *(const uint2*)(kvT + t);
          u16 zz[4];
#pragma unroll
          for (int j = 0; j < 4; ++j) {
            float y = nt ? acc1[4 * g + j] : acc0[4 * g + j];
            zz[j] = f2bf(rbf(y) + rbf(bf2f(ext4(kvv, j)) * Dd));
          }
          uint2 o;
          o.x = (unsigned)zz[0] | ((unsigned)zz[1] << 16);
          o.y = (unsigned)zz[2] | ((unsigned)zz[3] << 16);
          *(uint2*)(zT + t) = o;
        }
      }
    }
  }
}

__global__ __launch_bounds__(256) void k3_out(const u16* __restrict__ qin, const u16* __restrict__ wo,
                                             const char* __restrict__ ws, u16* __restrict__ out) {
  constexpr int AS3 = 1040;
  constexpr int BS3 = 144;
  __shared__ __attribute__((aligned(16))) char smem[64 * AS3 + 512 * BS3];
  const int tid = threadIdx.x;
  const int lane = tid & 63, wid = tid >> 6;
  const int r32 = lane & 31, hh = lane >> 5;
  const int bm = blockIdx.x >> 3, bn = blockIdx.x & 7;
  const u16* o = (const u16*)(ws + OFF_O);
  char* sA = smem;
  char* sB = smem + 64 * AS3;
  const int wm = wid >> 1, wn = wid & 1;
  f32x16 acc;
#pragma unroll
  for (int e = 0; e < 16; ++e) acc[e] = 0.f;
  {
    u32x4 ra[16], rb[16];
#pragma unroll
    for (int i = 0; i < 16; ++i) {
      int c = tid + 256 * i;
      int row = c >> 6, kc = c & 63;
      ra[i] = *(const u32x4*)(o + (size_t)(bm * 64 + row) * D + kc * 8);
      int krow = c >> 3, nc = c & 7;
      rb[i] = *(const u32x4*)(wo + (size_t)krow * D + bn * 64 + nc * 8);
    }
#pragma unroll
    for (int i = 0; i < 16; ++i) {
      int c = tid + 256 * i;
      int row = c >> 6, kc = c & 63;
      *(u32x4*)(sA + row * AS3 + kc * 16) = ra[i];
      int krow = c >> 3, nc = c & 7;
      *(u32x4*)(sB + krow * BS3 + nc * 16) = rb[i];
    }
  }
  __syncthreads();
  {
#pragma unroll
    for (int ks = 0; ks < 32; ++ks) {
      bf16x8 af = *(const bf16x8*)(sA + (wm * 32 + r32) * AS3 + (ks * 16 + 8 * hh) * 2);
      const int i16 = lane & 15, q4 = i16 >> 2, p4 = i16 & 3, g = lane >> 4;
      int colbase = wn * 32 + (g & 1) * 16;
      int krow = ks * 16 + 8 * hh + q4;
      unsigned a0 = (unsigned)(size_t)(sB + krow * BS3 + (colbase + 4 * p4) * 2);
      s16x4 lo = tr_read(a0);
      s16x4 hi = tr_read(a0 + 4 * BS3);
      bf16x8 bfr = __builtin_shufflevector(lo, hi, 0, 1, 2, 3, 4, 5, 6, 7);
      acc = MFMA32(bfr, af, acc);
    }
  }
  const u16* zT = (const u16*)(ws + OFF_ZT);
  const float* g = (const float*)(ws + OFF_G);
  const int t = bm * 64 + wm * 32 + r32;
  const float gg = g[t];
  const float omg = rbf(1.f - gg);
#pragma unroll
  for (int gq = 0; gq < 4; ++gq) {
    const int n = bn * 64 + wn * 32 + 8 * gq + 4 * hh;
    uint2 qv = *(const uint2*)(qin + (size_t)t * D + n);
    unsigned ov[2];
    u16 r4[4];
#pragma unroll
    for (int j = 0; j < 4; ++j) {
      float z = bf2f(zT[(size_t)(n + j) * S + t]);
      float a = rbf(bf2f(ext4(qv, j)) * z);
      float b = rbf(acc[4 * gq + j]);
      r4[j] = f2bf(rbf(gg * a) + rbf(omg * b));
    }
    uint2 ovv;
    ovv.x = (unsigned)r4[0] | ((unsigned)r4[1] << 16);
    ovv.y = (unsigned)r4[2] | ((unsigned)r4[3] << 16);
    *(uint2*)(out + (size_t)t * D + n) = ovv;
  }
}

extern "C" void kernel_launch(void* const* d_in, const int* in_sizes, int n_in, void* d_out, int out_size,
                              void* d_ws, size_t ws_size, hipStream_t stream) {
  const u16* A_q = (const u16*)d_in[0];
  const u16* A_k = (const u16*)d_in[1];
  const u16* A_v = (const u16*)d_in[2];
  const u16* A_shift_k = (const u16*)d_in[3];
  const u16* A_ssm_k = (const u16*)d_in[4];
  const u16* A_ssm_D = (const u16*)d_in[5];
  const u16* B_wqkv = (const u16*)d_in[7];
  const u16* B_wo = (const u16*)d_in[8];
  const u16* gate_w = (const u16*)d_in[9];
  char* ws = (char*)d_ws;
  k1_qkv_prep<<<K1_GEMM_BLOCKS + K1_PREP_BLOCKS, 256, 0, stream>>>(A_q, A_k, A_v, A_shift_k, B_wqkv, gate_w, ws);
  k2_attn_conv<<<K2_ATTN_BLOCKS + K2_CONV_BLOCKS, 512, 0, stream>>>(A_ssm_k, A_ssm_D, ws);
  k3_out<<<256, 256, 0, stream>>>(A_q, B_wo, ws, (u16*)d_out);
}
```
